```python
import torch

def _softmax(x, axis=-1):
    return torch.softmax(x, dim=axis)
def _gelu(x):
    return torch.nn.functional.gelu(x, approximate="tanh")
def _silu(x):
    return torch.nn.functional.silu(x)
def _sigmoid(x):
    return torch.sigmoid(x)
def _layernorm(x, axis=-1, eps=1e-5):
    m = x.mean(dim=axis, keepdim=True)
    v = x.var(dim=axis, keepdim=True, unbiased=False)
    return (x - m) / torch.sqrt(v + eps)
def _scatter_add(tgt, idx, src, axis=0):
    return tgt.clone().index_add_(axis, idx, src)
def _segment_sum(src, seg, n):
    out = torch.zeros((n,) + src.shape[1:], dtype=src.dtype)
    return out.index_add_(0, seg, src)
def _pad(x, pw):
    flat = tuple(v for pair in reversed(pw) for v in pair)
    return torch.nn.functional.pad(x, flat)

kernel_name = 'standalone__moe_arch_mod__prompt'

def setup_inputs():

    _w = (lambda a: (lambda t: t.to(torch.bfloat16) if t.is_floating_point() else t)(torch.as_tensor(a)))
    B, S, D, F, C = 1, 2048, 576, 4608, 2
    return {
        "x": _w((lambda *s: torch.randn(*s, dtype=torch.bfloat16) * 0.1)(B, S, D)),
        "w_r": _w((lambda *s: torch.randn(*s, dtype=torch.bfloat16) * 0.1)(D)),
        "w_gate": _w((lambda *s: torch.randn(*s, dtype=torch.bfloat16) * 0.1)(D, F)),
        "w_up": _w((lambda *s: torch.randn(*s, dtype=torch.bfloat16) * 0.1)(D, F)),
        "w_down": _w((lambda *s: torch.randn(*s, dtype=torch.bfloat16) * 0.1)(F, D)),
    }

def reference(x, w_r, w_gate, w_up, w_down):
    B, S, D = x.shape
    C = 2
    r = x @ w_r
    idx = torch.argsort(-r, axis=-1)[:, :C]
    idx_e = (lambda x, n, axis: torch.repeat_interleave(x, n, dim=axis))(idx.reshape(B, C, 1), D, axis=2)
    x_sel = (lambda x, i, axis: torch.take_along_dim(x, i, dim=axis))(x, idx_e, axis=1)
    g = _silu(x_sel @ w_gate)
    u = x_sel @ w_up
    y = (g * u) @ w_down
    r_sel = _sigmoid((lambda x, i, axis: torch.take_along_dim(x, i, dim=axis))(r, idx, axis=1))
    y = y * r_sel.reshape(B, C, 1)
    b_off = (torch.arange(B) * S).reshape(B, 1)
    flat_idx = (idx + b_off).reshape(B * C)
    zero = (x * 0.0).reshape(B * S, D)
    upd = _scatter_add(zero, flat_idx, y.reshape(B * C, D), axis=0)
    return x + upd.reshape(B, S, D)
```

```cpp
#include <hip/hip_runtime.h>
#include <hip/hip_bf16.h>
#include <stdint.h>

typedef unsigned v4u __attribute__((ext_vector_type(4)));
typedef unsigned v2u __attribute__((ext_vector_type(2)));
typedef float f32x4 __attribute__((ext_vector_type(4)));

constexpr int S = 2048, D = 576, F = 4608;

__device__ __forceinline__ float bflo(unsigned u) { return __builtin_bit_cast(float, u << 16); }
__device__ __forceinline__ float bfhi(unsigned u) { return __builtin_bit_cast(float, u & 0xffff0000u); }
__device__ __forceinline__ unsigned short f2bf(float f) {
  __hip_bfloat16 b = __float2bfloat16(f);
  return __builtin_bit_cast(unsigned short, b);
}
__device__ __forceinline__ float rbf(float f) { return bflo(f2bf(f)); }
__device__ __forceinline__ unsigned pack2(float a, float b) { return (unsigned)f2bf(a) | ((unsigned)f2bf(b) << 16); }

__global__ void __launch_bounds__(576) k_router(const v4u* __restrict__ x, const v4u* __restrict__ wr, v4u* __restrict__ out, float* __restrict__ scores) {
  __shared__ float part[576];
  int t = threadIdx.x;
  int tok = blockIdx.x * 8 + t / 72, c = t % 72;
  v4u xv = x[tok * 72 + c];
  v4u wv = wr[c];
  float s = 0.f;
  v4u o;
#pragma unroll
  for (int j = 0; j < 4; ++j) {
    float a0 = bflo(xv[j]), a1 = bfhi(xv[j]);
    s = fmaf(a0, bflo(wv[j]), s);
    s = fmaf(a1, bfhi(wv[j]), s);
    float z0 = a0 * 0.0f, z1 = a1 * 0.0f;
    o[j] = pack2(a0 + z0, a1 + z1);
  }
  out[tok * 72 + c] = o;
  part[t] = s;
  __syncthreads();
  if (t < 8) {
    float a = 0.f;
    for (int j = 0; j < 72; ++j) a += part[t * 72 + j];
    scores[blockIdx.x * 8 + t] = rbf(a);
  }
}

__device__ __forceinline__ unsigned keyof(float r, int idx) {
  r = r + 0.0f;
  unsigned b = __builtin_bit_cast(unsigned, r) >> 16;
  unsigned k = (b & 0x8000u) ? (~b & 0xffffu) : (b | 0x8000u);
  if (r != r) k = 0;
  return (k << 16) | (unsigned)(2047 - idx);
}

__global__ void __launch_bounds__(576) k_mlp(const unsigned short* __restrict__ x, const float* __restrict__ scores,
    const v4u* __restrict__ wg, const v4u* __restrict__ wu, const v4u* __restrict__ wd, float* __restrict__ slab, int* __restrict__ sel) {
  __shared__ unsigned red[32];
  __shared__ float xs[2][D];
  __shared__ float gp[9][128];
  __shared__ float hs[2][32];
  int t = threadIdx.x, lane = t & 63, w = t >> 6;
  int b = blockIdx.x;
  int cg = (b % 8) * 18 + b / 8;
  unsigned k1 = 0, k2 = 0;
  for (int i = t; i < S; i += 576) {
    unsigned k = keyof(scores[i], i);
    if (k > k1) { k2 = k1; k1 = k; } else if (k > k2) k2 = k;
  }
  for (int o = 32; o >= 1; o >>= 1) {
    unsigned a1 = __shfl_xor(k1, o), a2 = __shfl_xor(k2, o);
    unsigned n1 = max(k1, a1), n2 = max(min(k1, a1), max(k2, a2));
    k1 = n1; k2 = n2;
  }
  if (lane == 0) { red[2 * w] = k1; red[2 * w + 1] = k2; }
  __syncthreads();
  k1 = 0; k2 = 0;
  for (int i = 0; i < 18; ++i) { unsigned k = red[i]; if (k > k1) { k2 = k1; k1 = k; } else if (k > k2) k2 = k; }
  int i1 = 2047 - (k1 & 0xffff), i2 = 2047 - (k2 & 0xffff);
  if (b == 0 && t == 0) { sel[0] = i1; sel[1] = i2; }
  xs[0][t] = bflo(x[i1 * D + t]);
  xs[1][t] = bflo(x[i2 * D + t]);
  __syncthreads();
  float acc[2][2][8];
#pragma unroll
  for (int m = 0; m < 2; ++m) for (int q = 0; q < 2; ++q) for (int j = 0; j < 8; ++j) acc[m][q][j] = 0.f;
#pragma unroll
  for (int i = 0; i < 4; ++i) {
    int row = 64 * w + 16 * i + (lane >> 2);
    v4u g = wg[row * (F / 8) + cg * 4 + (lane & 3)];
    v4u u = wu[row * (F / 8) + cg * 4 + (lane & 3)];
    float x0 = xs[0][row], x1 = xs[1][row];
#pragma unroll
    for (int j = 0; j < 4; ++j) {
      acc[0][0][2 * j] = fmaf(x0, bflo(g[j]), acc[0][0][2 * j]);
      acc[0][0][2 * j + 1] = fmaf(x0, bfhi(g[j]), acc[0][0][2 * j + 1]);
      acc[0][1][2 * j] = fmaf(x1, bflo(g[j]), acc[0][1][2 * j]);
      acc[0][1][2 * j + 1] = fmaf(x1, bfhi(g[j]), acc[0][1][2 * j + 1]);
      acc[1][0][2 * j] = fmaf(x0, bflo(u[j]), acc[1][0][2 * j]);
      acc[1][0][2 * j + 1] = fmaf(x0, bfhi(u[j]), acc[1][0][2 * j + 1]);
      acc[1][1][2 * j] = fmaf(x1, bflo(u[j]), acc[1][1][2 * j]);
      acc[1][1][2 * j + 1] = fmaf(x1, bfhi(u[j]), acc[1][1][2 * j + 1]);
    }
  }
#pragma unroll
  for (int m = 0; m < 2; ++m) for (int q = 0; q < 2; ++q) for (int j = 0; j < 8; ++j) {
    float v = acc[m][q][j];
    v += __shfl_xor(v, 4); v += __shfl_xor(v, 8); v += __shfl_xor(v, 16); v += __shfl_xor(v, 32);
    if (lane < 4) gp[w][(m * 2 + q) * 32 + lane * 8 + j] = v;
  }
  __syncthreads();
  if (t < 64) {
    int q = t >> 5, c = t & 31;
    float g = 0.f, u = 0.f;
    for (int i = 0; i < 9; ++i) { g += gp[i][q * 32 + c]; u += gp[i][(2 + q) * 32 + c]; }
    g = rbf(g); u = rbf(u);
    float sg = rbf(g / (1.0f + expf(-g)));
    hs[q][c] = rbf(sg * u);
  }
  __syncthreads();
  float y[2][8];
  for (int q = 0; q < 2; ++q) for (int j = 0; j < 8; ++j) y[q][j] = 0.f;
#pragma unroll
  for (int i = 0; i < 4; ++i) {
    int r = 8 * i + (lane >> 3);
    v4u d = wd[(cg * 32 + r) * 72 + w * 8 + (lane & 7)];
    float h0 = hs[0][r], h1 = hs[1][r];
#pragma unroll
    for (int j = 0; j < 4; ++j) {
      y[0][2 * j] = fmaf(h0, bflo(d[j]), y[0][2 * j]);
      y[0][2 * j + 1] = fmaf(h0, bfhi(d[j]), y[0][2 * j + 1]);
      y[1][2 * j] = fmaf(h1, bflo(d[j]), y[1][2 * j]);
      y[1][2 * j + 1] = fmaf(h1, bfhi(d[j]), y[1][2 * j + 1]);
    }
  }
#pragma unroll
  for (int q = 0; q < 2; ++q) for (int j = 0; j < 8; ++j) {
    float v = y[q][j];
    v += __shfl_xor(v, 8); v += __shfl_xor(v, 16); v += __shfl_xor(v, 32);
    if (lane < 8) slab[(size_t)b * 1152 + q * 576 + w * 64 + lane * 8 + j] = v;
  }
}

__global__ void __launch_bounds__(256) k_final(const unsigned short* __restrict__ x, const float* __restrict__ scores, const float* __restrict__ slab,
    const int* __restrict__ sel, unsigned short* __restrict__ out) {
  __shared__ float p[16][17];
  int t = threadIdx.x, o = blockIdx.x * 16 + (t & 15), part = t >> 4;
  float a = 0.f;
#pragma unroll
  for (int i = 0; i < 9; ++i) a += slab[(size_t)(part * 9 + i) * 1152 + o];
  p[part][t & 15] = a;
  __syncthreads();
  if (t < 16) {
    float y = 0.f;
    for (int i = 0; i < 16; ++i) y += p[i][t];
    int q = o / 576, d = o % 576;
    int tok = sel[q];
    float r = scores[tok];
    float sg = rbf(1.0f / (1.0f + expf(-r)));
    y = rbf(rbf(y) * sg);
    float xv = bflo(x[tok * D + d]);
    float z = xv * 0.0f;
    float upd = rbf(z + y);
    out[tok * D + d] = f2bf(xv + upd);
  }
}

extern "C" void kernel_launch(void* const* d_in, const int* in_sizes, int n_in,
                              void* d_out, int out_size, void* d_ws, size_t ws_size,
                              hipStream_t stream) {
  const unsigned short* x = (const unsigned short*)d_in[0];
  float* scores = (float*)d_ws;
  int* sel = (int*)((char*)d_ws + 8192);
  float* slab = (float*)((char*)d_ws + 16384);
  k_router<<<256, 576, 0, stream>>>((const v4u*)x, (const v4u*)d_in[1], (v4u*)d_out, scores);
  k_mlp<<<144, 576, 0, stream>>>(x, scores, (const v4u*)d_in[2], (const v4u*)d_in[3], (const v4u*)d_in[4], slab, sel);
  k_final<<<72, 256, 0, stream>>>(x, scores, slab, sel, (unsigned short*)d_out);
}
```

```cpp
#include <hip/hip_runtime.h>
#include <hip/hip_bf16.h>
#include <stdint.h>

#define GAS __attribute__((address_space(1)))
#define LAS __attribute__((address_space(3)))
typedef unsigned v4u __attribute__((ext_vector_type(4)));
typedef unsigned v2u __attribute__((ext_vector_type(2)));
typedef float f32x4 __attribute__((ext_vector_type(4)));
typedef float f32x2 __attribute__((ext_vector_type(2)));
typedef short v4s __attribute__((ext_vector_type(4)));
typedef __bf16 bf16x8 __attribute__((ext_vector_type(8)));
typedef __bf16 bf16x2 __attribute__((ext_vector_type(2)));
typedef unsigned long long u64;

constexpr int S = 2048, D = 576, F = 4608;
constexpr int NWG = 256, NT = 576, NMLP = 144, NRED = 72;
constexpr unsigned WS_EPOCH = 0, WS_GRAN = 4096, WS_SLAB = 16384;
constexpr unsigned SLAB_BYTES = NRED * NMLP * 64;
constexpr unsigned WS_BYTES = WS_SLAB + SLAB_BYTES;
constexpr unsigned POISON = 0x7F81DEADu;
constexpr unsigned L_W = 0;
constexpr unsigned L_P = 9 * 12288;
constexpr unsigned L_R = L_P + 9 * 512;
constexpr unsigned L_S = L_R + 8 * 48;
constexpr unsigned L_BYTES = L_S + 16;
constexpr unsigned SPIN_CAP = 1u << 18;

#define ST(k)

__device__ __forceinline__ float bflo(unsigned u) { return __builtin_bit_cast(float, u << 16); }
__device__ __forceinline__ float bfhi(unsigned u) { return __builtin_bit_cast(float, u & 0xffff0000u); }
__device__ __forceinline__ unsigned pk2(float a, float b) {
  bf16x2 v = {(__bf16)a, (__bf16)b};
  return __builtin_bit_cast(unsigned, v);
}
__device__ __forceinline__ float rbf(float a) { return bflo(pk2(a, a)); }
template <int CTRL> __device__ __forceinline__ unsigned dppu(unsigned v) { return (unsigned)__builtin_amdgcn_update_dpp(0, (int)v, CTRL, 0xf, 0xf, true); }
template <int CTRL> __device__ __forceinline__ float dppf(float v) { return __builtin_bit_cast(float, dppu<CTRL>(__builtin_bit_cast(unsigned, v))); }
constexpr int QP_X1 = 0xB1, QP_X2 = 0x4E, ROW_HMIRROR = 0x141, ROW_MIRROR = 0x140;
constexpr int ROW_SHL = 0x100, ROW_ROR = 0x120;
__device__ __forceinline__ unsigned wave_max_u32(unsigned v) {
  v = max(v, dppu<QP_X1>(v));
  v = max(v, dppu<QP_X2>(v));
  v = max(v, dppu<ROW_HMIRROR>(v));
  v = max(v, dppu<ROW_MIRROR>(v));
  unsigned a = __builtin_amdgcn_readlane(v, 0), b = __builtin_amdgcn_readlane(v, 16), c = __builtin_amdgcn_readlane(v, 32), d = __builtin_amdgcn_readlane(v, 48);
  return max(max(a, b), max(c, d));
}
__device__ __forceinline__ f32x4 mfma16(v4u a, v4u b, f32x4 c) {
  return __builtin_amdgcn_mfma_f32_16x16x32_bf16(__builtin_bit_cast(bf16x8, a), __builtin_bit_cast(bf16x8, b), c, 0, 0, 0);
}
__device__ __forceinline__ v2u trd(unsigned a) {
  return __builtin_bit_cast(v2u, __builtin_amdgcn_ds_read_tr16_b64_v4i16((LAS v4s*)a));
}
__device__ __forceinline__ v4u trd2(unsigned a, unsigned step) { v2u lo = trd(a), hi = trd(a + step); return v4u{lo.x, lo.y, hi.x, hi.y}; }
#define WNT " nt"
__device__ __forceinline__ void dma16(unsigned voff, const void* base, unsigned lds) {
  asm volatile("s_mov_b32 m0, %2\n\ts_nop 0\n\tglobal_load_lds_dwordx4 %0, %1" WNT :: "v"(voff), "s"(base), "s"(lds) : "memory");
}
#define BAR() do { asm volatile("s_waitcnt lgkmcnt(0)\n\ts_barrier" ::: "memory"); } while (0)

__global__ void __launch_bounds__(NT, 3) moe_kernel(const unsigned char* __restrict__ x, const unsigned char* __restrict__ wr,
    const unsigned char* __restrict__ wg, const unsigned char* __restrict__ wu, const unsigned char* __restrict__ wd,
    unsigned char* __restrict__ out, unsigned char* __restrict__ ws) {
  __shared__ __attribute__((aligned(16))) unsigned char smem[L_BYTES];
  ST(0);
  asm volatile("" :: "s"(x), "s"(wr), "s"(wg), "s"(wu), "s"(wd), "s"(out), "s"(ws));
  const unsigned lbase = (unsigned)(size_t)(LAS unsigned char*)smem;
  const int tid = threadIdx.x, lane = tid & 63, w = __builtin_amdgcn_readfirstlane(tid >> 6), b = blockIdx.x;
  const bool is_mlp = b < NMLP, is_red = !is_mlp && b < NMLP + NRED;
  const __amdgpu_buffer_rsrc_t rs = __builtin_amdgcn_make_buffer_rsrc((void*)ws, 0, (int)WS_BYTES, 0x00020000);

  const unsigned xo = (unsigned)((8 * b + (lane >> 3)) * (D * 2) + (8 * w + (lane & 7)) * 16);
  v4u xv, wv; v2u ep;
  asm volatile("global_load_dwordx4 %0, %1, %2" : "=v"(xv) : "v"(xo), "s"(x));
  asm volatile("global_load_dwordx4 %0, %1, %2" : "=v"(wv) : "v"((unsigned)((8 * w + (lane & 7)) * 16)), "s"(wr));
  asm volatile("global_load_dwordx2 %0, %1, %2 sc1" : "=v"(ep) : "v"(0u), "s"(ws));
  const int cg = (b & 7) * 18 + (b >> 3);
  const unsigned lw = lbase + L_W + w * 12288;
  if (is_mlp) {
    const unsigned vo = (unsigned)((lane >> 2) * (F * 2) + (((lane & 3) ^ ((lane >> 5) << 1)) << 4));
    const size_t go = (size_t)(64 * w) * (F * 2) + cg * 64;
#pragma unroll
    for (int i = 0; i < 4; ++i) dma16(vo, wg + go + (size_t)i * 16 * (F * 2), lw + i * 1024);
#pragma unroll
    for (int i = 0; i < 4; ++i) dma16(vo, wu + go + (size_t)i * 16 * (F * 2), lw + 4096 + i * 1024);
    const unsigned s0 = (lane >> 4) & 1;
    const unsigned vd0 = (unsigned)((lane >> 3) * (D * 2) + (((lane & 7) ^ (s0 << 1)) << 4));
    const unsigned vd1 = (unsigned)((lane >> 3) * (D * 2) + (((lane & 7) ^ ((s0 | 2) << 1)) << 4));
    const size_t dn = (size_t)(32 * cg) * (D * 2) + w * 128;
#pragma unroll
    for (int i = 0; i < 4; ++i) dma16((i & 1) ? vd1 : vd0, wd + dn + (size_t)i * 8 * (D * 2), lw + 8192 + i * 1024);
    asm volatile("s_waitcnt vmcnt(12)" ::: "memory");
  } else {
    if (is_red) {
      const v4u pz = {POISON, POISON, POISON, POISON};
      __builtin_amdgcn_raw_buffer_store_b128(pz, rs, (int)(WS_SLAB + (b - NMLP) * (NMLP * 64) + tid * 16), 0, 16);
    }
    asm volatile("s_waitcnt vmcnt(0)" ::: "memory");
  }
  asm volatile("; operands landed" : "+v"(xv), "+v"(wv), "+v"(ep) :: "memory");
  ST(1);
  {
    float s = 0.f;
#pragma unroll
    for (int j = 0; j < 4; ++j) { s = fmaf(bflo(xv[j]), bflo(wv[j]), s); s = fmaf(bfhi(xv[j]), bfhi(wv[j]), s); }
    s += dppf<QP_X1>(s); s += dppf<QP_X2>(s); s += dppf<ROW_HMIRROR>(s);
    if ((lane & 7) == 0) *(LAS float*)(lbase + L_R + (lane >> 3) * 48 + w * 4) = s;
  }
  v4u ov;
#pragma unroll
  for (int j = 0; j < 4; ++j) { const float a = bflo(xv[j]), c = bfhi(xv[j]); ov[j] = pk2(a + a * 0.0f, c + c * 0.0f); }
  BAR();
  ST(2);
  const u64 epoch = (((u64)ep.y << 32) | ep.x) + 1;
  const u64 tagv = epoch << 19;
  if (w == 0) {
    const LAS f32x4* rp = (const LAS f32x4*)(lbase + L_R + (lane & 7) * 48);
    const f32x4 a = rp[0], c = rp[1]; const float e = *(const LAS float*)(rp + 2);
    const float sum = ((((a.x + a.y) + (a.z + a.w)) + ((c.x + c.y) + (c.z + c.w))) + e);
    const float rr = rbf(sum) + 0.0f;
    const unsigned bits = __builtin_bit_cast(unsigned, rr) >> 16;
    unsigned k16 = (bits & 0x8000u) ? (~bits & 0xffffu) : (bits | 0x8000u);
    if (rr != rr) k16 = 0;
    const unsigned key = (k16 << 3) | (7 - (lane & 7));
    unsigned m1 = max(key, dppu<QP_X1>(key)); m1 = max(m1, dppu<QP_X2>(m1)); m1 = max(m1, dppu<ROW_HMIRROR>(m1));
    const unsigned k2 = key == m1 ? 0u : key;
    unsigned m2 = max(k2, dppu<QP_X1>(k2)); m2 = max(m2, dppu<QP_X2>(m2)); m2 = max(m2, dppu<ROW_HMIRROR>(m2));
    if (lane < 2) {
      const u64 g = tagv | (u64)(lane == 0 ? m1 : m2);
      __hip_atomic_store((GAS u64*)(ws + WS_GRAN + b * 16 + lane * 8), g, __ATOMIC_RELAXED, __HIP_MEMORY_SCOPE_AGENT);
    }
    ST(3);
    const unsigned tlo = (unsigned)tagv >> 19, thi = (unsigned)(tagv >> 32);
    v4u g[4];
    for (unsigned spins = 0;; ++spins) {
      bool ok = true;
      asm volatile("" ::: "memory");
#pragma unroll
      for (int i = 0; i < 4; ++i) {
        g[i] = __builtin_amdgcn_raw_buffer_load_b128(rs, (int)(WS_GRAN + (lane + 64 * i) * 16), 0, 16);
      }
#pragma unroll
      for (int i = 0; i < 4; ++i) ok &= (g[i].y == thi) & (g[i].w == thi) & ((g[i].x >> 19) == tlo) & ((g[i].z >> 19) == tlo);
      if (__all(ok) || spins > SPIN_CAP) break;
    }
    unsigned k1 = 0, k2b = 0;
#pragma unroll
    for (int i = 0; i < 4; ++i) {
      const unsigned basei = 2040u - 8u * (lane + 64 * i);
      const unsigned p1 = g[i].x & 0x7ffffu, p2 = g[i].z & 0x7ffffu;
      const unsigned a = ((p1 >> 3) << 16) | (basei + (p1 & 7)), c = ((p2 >> 3) << 16) | (basei + (p2 & 7));
      const unsigned n1 = max(k1, a), n2 = max(min(k1, a), max(k2b, c));
      k1 = n1; k2b = n2;
    }
    const unsigned M1 = wave_max_u32(k1);
    const unsigned M2 = wave_max_u32(k1 == M1 ? k2b : k1);
    if (lane == 0) *(LAS v2u*)(lbase + L_S) = v2u{M1, M2};
    if (b == 0 && lane == 0) __hip_atomic_store((GAS u64*)(ws + WS_EPOCH), epoch, __ATOMIC_RELAXED, __HIP_MEMORY_SCOPE_AGENT);
    ST(4);
  }
  v4u ag[2][2][2], ad[4];
  const unsigned G = lane >> 4, qp = (lane & 15) >> 2, pp = lane & 3;
  auto read_frags = [&]() {
    const unsigned a0 = lw + (8 * G + qp) * 64 + (((pp >> 1) ^ (2 * (G & 1))) << 4) + 8 * (pp & 1);
#pragma unroll
    for (int m = 0; m < 2; ++m)
#pragma unroll
      for (int t = 0; t < 2; ++t)
#pragma unroll
        for (int s = 0; s < 2; ++s) ag[m][t][s] = trd2((a0 ^ (t << 5)) + m * 4096 + s * 2048, 256);
    const unsigned swz = ((qp >> 1) & 1) | ((G & 1) << 1);
    const unsigned d0 = lw + 8192 + (8 * G + qp) * 128 + (swz << 5) + ((pp >> 1) << 4) + 8 * (pp & 1);
#pragma unroll
    for (int t = 0; t < 4; ++t) ad[t] = trd2(d0 ^ (t << 5), 512);
  };
  if (is_mlp && w != 0) {
    asm volatile("s_waitcnt vmcnt(0)" ::: "memory");
    if (w == 1) { ST(11); }
    read_frags();
  }
  BAR();
  ST(5);
  const v2u mk = *(const LAS v2u*)(lbase + L_S);
  const int i1 = 2047 - (int)(mk.x & 0xffffu), i2 = 2047 - (int)(mk.y & 0xffffu);
  const int mytok = 8 * b + (lane >> 3);
  if (is_mlp) {
    const int tok = (lane & 1) ? i2 : i1;
    const GAS v4u* xp = (const GAS v4u*)(x + tok * (D * 2) + (64 * w + 8 * G) * 2);
    const v4u bx0 = xp[0], bx1 = xp[4];
    if (mytok != i1 && mytok != i2) *(GAS v4u*)(out + xo) = ov;
    if (w == 0) read_frags();
    f32x4 acc[2][2];
#pragma unroll
    for (int m = 0; m < 2; ++m)
#pragma unroll
      for (int t = 0; t < 2; ++t) { acc[m][t] = mfma16(ag[m][t][0], bx0, f32x4{0.f, 0.f, 0.f, 0.f}); acc[m][t] = mfma16(ag[m][t][1], bx1, acc[m][t]); }
    ST(6);
    if ((lane & 15) < 2) {
#pragma unroll
      for (int t = 0; t < 2; ++t) {
        LAS f32x4* pw = (LAS f32x4*)(lbase + L_P + w * 512 + ((lane & 1) * 32 + 16 * t + 4 * G) * 8);
        pw[0] = f32x4{acc[0][t][0], acc[1][t][0], acc[0][t][1], acc[1][t][1]};
        pw[1] = f32x4{acc[0][t][2], acc[1][t][2], acc[0][t][3], acc[1][t][3]};
      }
    }
    BAR();
    ST(7);
    const LAS f32x2* pr = (const LAS f32x2*)(lbase + L_P + ((lane & 1) * 32 + 8 * G + ((lane >> 1) & 7)) * 8);
    f32x2 pv[9];
#pragma unroll
    for (int i = 0; i < 9; ++i) pv[i] = pr[i * 64];
    const f32x2 sgu = (((pv[0] + pv[1]) + (pv[2] + pv[3])) + ((pv[4] + pv[5]) + (pv[6] + pv[7]))) + pv[8];
    const float gg = rbf(sgu.x), uu = rbf(sgu.y);
    const float sg = rbf(gg / (1.0f + __expf(-gg)));
    const unsigned hb = pk2(sg * uu, 0.f) & 0xffffu;
    const unsigned hp = hb | (dppu<QP_X2>(hb) << 16);
    const v4u bh = {hp, dppu<ROW_SHL + 4>(hp), dppu<ROW_SHL + 8>(hp), dppu<ROW_SHL + 12>(hp)};
    f32x4 y[4];
#pragma unroll
    for (int t = 0; t < 4; ++t) y[t] = mfma16(ad[t], bh, f32x4{0.f, 0.f, 0.f, 0.f});
    if ((lane & 15) < 2) {
      const unsigned so = WS_SLAB + (((lane & 1) * 36 + 4 * w) * NMLP + b) * 64 + G * 16;
#pragma unroll
      for (int t = 0; t < 4; ++t) __builtin_amdgcn_raw_buffer_store_b128(__builtin_bit_cast(v4u, y[t]), rs, (int)(so + t * NMLP * 64), 0, 16);
    }
    ST(8);
  } else {
    if (mytok != i1 && mytok != i2) *(GAS v4u*)(out + xo) = ov;
    if (is_red) {
      const int r = b - NMLP, q = r / 36, d0 = (r % 36) * 16;
      const unsigned mkq = q ? mk.y : mk.x;
      const int tok = q ? i2 : i1;
      float xe = 0.f;
      if (tid < 16) xe = bflo(*(const GAS unsigned short*)(x + tok * (D * 2) + (d0 + tid) * 2));
      v4u pv;
      for (unsigned spins = 0;; ++spins) {
        asm volatile("" ::: "memory");
        pv = __builtin_amdgcn_raw_buffer_load_b128(rs, (int)(WS_SLAB + r * (NMLP * 64) + tid * 16), 0, 16);
        const bool ok = (pv.x != POISON) & (pv.y != POISON) & (pv.z != POISON) & (pv.w != POISON);
        if (__all(ok) || spins > SPIN_CAP) break;
      }
      ST(9);
      auto s01 = __builtin_amdgcn_permlane16_swap(pv.x, pv.y, false, false);
      auto s23 = __builtin_amdgcn_permlane16_swap(pv.z, pv.w, false, false);
      const float a01 = __builtin_bit_cast(float, (unsigned)s01[0]) + __builtin_bit_cast(float, (unsigned)s01[1]);
      const float a23 = __builtin_bit_cast(float, (unsigned)s23[0]) + __builtin_bit_cast(float, (unsigned)s23[1]);
      auto s4 = __builtin_amdgcn_permlane32_swap(__builtin_bit_cast(unsigned, a01), __builtin_bit_cast(unsigned, a23), false, false);
      float v = __builtin_bit_cast(float, (unsigned)s4[0]) + __builtin_bit_cast(float, (unsigned)s4[1]);
      v += dppf<ROW_ROR + 4>(v);
      v += dppf<ROW_ROR + 8>(v);
      if ((lane & 15) < 4) *(LAS float*)(lbase + L_P + w * 64 + ((lane & 3) * 4 + G) * 4) = v;
      BAR();
      if (tid < 16) {
        float p[9];
#pragma unroll
        for (int i = 0; i < 9; ++i) p[i] = *(const LAS float*)(lbase + L_P + i * 64 + tid * 4);
        const float yy = (((p[0] + p[1]) + (p[2] + p[3])) + ((p[4] + p[5]) + (p[6] + p[7]))) + p[8];
        const unsigned k16 = mkq >> 16;
        const float rr = bflo((k16 & 0x8000u) ? (k16 & 0x7fffu) : (~k16 & 0xffffu));
        const float sig = rbf(1.0f / (1.0f + __expf(-rr)));
        const float ys = rbf(rbf(yy) * sig);
        const float upd = rbf(xe * 0.0f + ys);
        *(GAS unsigned short*)(out + tok * (D * 2) + (d0 + tid) * 2) = (unsigned short)pk2(xe + upd, 0.f);
      }
    }
  }
}

extern "C" void kernel_launch(void* const* d_in, const int* in_sizes, int n_in,
                              void* d_out, int out_size, void* d_ws, size_t ws_size,
                              hipStream_t stream) {
  moe_kernel<<<NWG, NT, 0, stream>>>((const unsigned char*)d_in[0], (const unsigned char*)d_in[1], (const unsigned char*)d_in[2],
      (const unsigned char*)d_in[3], (const unsigned char*)d_in[4], (unsigned char*)d_out, (unsigned char*)d_ws);
}
```

```cpp
#include <hip/hip_runtime.h>
#include <hip/hip_bf16.h>
#include <stdint.h>

#define GAS __attribute__((address_space(1)))
#define LAS __attribute__((address_space(3)))
typedef unsigned v4u __attribute__((ext_vector_type(4)));
typedef unsigned v2u __attribute__((ext_vector_type(2)));
typedef float f32x4 __attribute__((ext_vector_type(4)));
typedef float f32x2 __attribute__((ext_vector_type(2)));
typedef short v4s __attribute__((ext_vector_type(4)));
typedef __bf16 bf16x8 __attribute__((ext_vector_type(8)));
typedef __bf16 bf16x2 __attribute__((ext_vector_type(2)));
typedef unsigned long long u64;

constexpr int D = 576, F = 4608;
constexpr int NWG = 256, NT = 704, NMLP = 144, NRT = 128, NRED = 36;
constexpr int NRDY = NRED + NMLP - NRT;
constexpr unsigned GSP = 32;
constexpr unsigned WS_EPOCH = 0, WS_RDY = 1024, WS_GRAN = 4096, WS_SLAB = 16384;
constexpr unsigned SLAB_BYTES = NRED * NMLP * 128;
constexpr unsigned WS_BYTES = WS_SLAB + SLAB_BYTES;
constexpr unsigned POISON = 0x7F81DEADu;
constexpr unsigned L_W = 0;
constexpr unsigned L_P = 9 * 12288;
constexpr unsigned L_S = L_P + 9 * 512;
constexpr unsigned L_BYTES = L_S + 16;
constexpr unsigned SPIN_CAP = 1u << 18;
constexpr int CODE_LINES = 60;
constexpr int GDELAY = 8;
constexpr int SDELAY = 112;

__device__ __forceinline__ float bflo(unsigned u) { return __builtin_bit_cast(float, u << 16); }
__device__ __forceinline__ float bfhi(unsigned u) { return __builtin_bit_cast(float, u & 0xffff0000u); }
__device__ __forceinline__ unsigned pk2(float a, float b) {
  bf16x2 v = {(__bf16)a, (__bf16)b};
  return __builtin_bit_cast(unsigned, v);
}
__device__ __forceinline__ float rbf(float a) { return bflo(pk2(a, a)); }
__device__ __forceinline__ float dot2(unsigned a, unsigned b, float c) {
  return __builtin_amdgcn_fdot2_f32_bf16(__builtin_bit_cast(bf16x2, a), __builtin_bit_cast(bf16x2, b), c, false);
}
__device__ __forceinline__ float sigm(float v) { return __builtin_amdgcn_rcpf(1.0f + __expf(-v)); }
template <int CTRL> __device__ __forceinline__ unsigned dppu(unsigned v) { return (unsigned)__builtin_amdgcn_update_dpp(0, (int)v, CTRL, 0xf, 0xf, true); }
template <int CTRL> __device__ __forceinline__ float dppf(float v) { return __builtin_bit_cast(float, dppu<CTRL>(__builtin_bit_cast(unsigned, v))); }
template <int CTRL, int BM> __device__ __forceinline__ float dppm(float old, float src) {
  return __builtin_bit_cast(float, __builtin_amdgcn_update_dpp(__builtin_bit_cast(int, old), __builtin_bit_cast(int, src), CTRL, 0xf, BM, false));
}
constexpr int QP_X1 = 0xB1, QP_X2 = 0x4E, ROW_HMIRROR = 0x141, ROW_MIRROR = 0x140;
constexpr int ROW_SHL = 0x100, ROW_SHR = 0x110, ROW_ROR = 0x120;
template <int CTRL> __device__ __forceinline__ void top2_dpp(unsigned& k1, unsigned& k2) {
  const unsigned p1 = dppu<CTRL>(k1), p2 = dppu<CTRL>(k2);
  const unsigned n2 = max(min(k1, p1), max(k2, p2));
  k1 = max(k1, p1); k2 = n2;
}
template <int W> __device__ __forceinline__ void top2_swap(unsigned& k1, unsigned& k2) {
  auto a = W == 16 ? __builtin_amdgcn_permlane16_swap(k1, k1, false, false) : __builtin_amdgcn_permlane32_swap(k1, k1, false, false);
  auto c = W == 16 ? __builtin_amdgcn_permlane16_swap(k2, k2, false, false) : __builtin_amdgcn_permlane32_swap(k2, k2, false, false);
  const unsigned a0 = a[0], a1 = a[1], c0 = c[0], c1 = c[1];
  k1 = max(a0, a1); k2 = max(min(a0, a1), max(c0, c1));
}
__device__ __forceinline__ f32x4 mfma16(v4u a, v4u b, f32x4 c) {
  return __builtin_amdgcn_mfma_f32_16x16x32_bf16(__builtin_bit_cast(bf16x8, a), __builtin_bit_cast(bf16x8, b), c, 0, 0, 0);
}
__device__ __forceinline__ v2u trd(unsigned a) {
  return __builtin_bit_cast(v2u, __builtin_amdgcn_ds_read_tr16_b64_v4i16((LAS v4s*)a));
}
__device__ __forceinline__ v4u trd2(unsigned a, unsigned step) { v2u lo = trd(a), hi = trd(a + step); return v4u{lo.x, lo.y, hi.x, hi.y}; }
__device__ __forceinline__ void dma16(unsigned voff, const void* base, unsigned lds) {
  asm volatile("s_mov_b32 m0, %2\n\ts_nop 0\n\tglobal_load_lds_dwordx4 %0, %1" :: "v"(voff), "s"(base), "s"(lds) : "memory");
}
__device__ __forceinline__ void touch(const void* p, unsigned lds) {
  asm volatile("s_mov_b32 m0, %1\n\ts_nop 0\n\tglobal_load_lds_dword %0, off" :: "v"(p), "s"(lds) : "memory");
}
__device__ __forceinline__ void st16(unsigned voff, void* base, v4u v) {
  asm volatile("global_store_dwordx4 %0, %1, %2 sc1\n\ts_nop 1" :: "v"(voff), "v"(v), "s"(base) : "memory");
}
#define VMW(n) asm volatile("s_waitcnt vmcnt(" #n ")" ::: "memory")
#define BAR() asm volatile("s_waitcnt lgkmcnt(0)\n\ts_barrier" ::: "memory")
#define RAWBAR() asm volatile("s_barrier" ::: "memory")
#define PINV(x) asm volatile("" : "+v"(x))
#define PINS(x) asm volatile("" : "+s"(x))
#define OPQ(v) ({ int t_ = (v); asm volatile("" : "+s"(t_)); t_; })

__device__ __forceinline__ void gather_top2(__amdgpu_buffer_rsrc_t rs, int lane, unsigned explo, unsigned thi, unsigned& k1, unsigned& k2) {
  v4u g[4];
  const int ro = (int)(WS_RDY + min(lane, NRDY / 2 - 1) * 16), go = (int)(WS_GRAN + lane * GSP);
  for (unsigned spins = 0;; ++spins) {
    asm volatile("" ::: "memory");
    const v4u rd = __builtin_amdgcn_raw_buffer_load_b128(rs, ro, 0, 16);
#pragma unroll
    for (int i = 0; i < 4; ++i) g[i] = __builtin_amdgcn_raw_buffer_load_b128(rs, go, i * 64 * GSP, 16);
    unsigned bad = (rd.y ^ thi) | (rd.w ^ thi) | (rd.x ^ explo) | (rd.z ^ explo);
#pragma unroll
    for (int i = 0; i < 4; ++i) bad |= (g[i].y ^ thi) | (g[i].w ^ thi) | (((g[i].x ^ explo) | (g[i].z ^ explo)) >> 27);
    if (__builtin_amdgcn_ballot_w64(bad != 0) == 0 || spins > SPIN_CAP) break;
  }
  k1 = g[0].x; k2 = g[0].z;
#pragma unroll
  for (int i = 1; i < 4; ++i) { const unsigned n2 = max(min(k1, g[i].x), max(k2, g[i].z)); k1 = max(k1, g[i].x); k2 = n2; }
  top2_dpp<QP_X1>(k1, k2); top2_dpp<QP_X2>(k1, k2); top2_dpp<ROW_HMIRROR>(k1, k2); top2_dpp<ROW_MIRROR>(k1, k2);
  top2_swap<16>(k1, k2); top2_swap<32>(k1, k2);
}
__device__ __forceinline__ void write_sel(unsigned lbase, int lane, unsigned k1, unsigned k2) {
  if (lane == 0) *(LAS v4u*)(lbase + L_S) = v4u{(2047u - (k1 & 0x7ffu)) * (D * 2), (2047u - (k2 & 0x7ffu)) * (D * 2), k1, k2};
}

__global__ void __launch_bounds__(NT, 3) standalone__moe_arch_mod__prompt_kernel(const unsigned char* x, const unsigned char* wr,
    const unsigned char* __restrict__ wg, const unsigned char* __restrict__ wu, const unsigned char* __restrict__ wd,
    unsigned char* __restrict__ out, unsigned char* __restrict__ ws) {
  __shared__ __attribute__((aligned(16))) unsigned char smem[L_BYTES];
  const unsigned lbase = (unsigned)(size_t)(LAS unsigned char*)smem;
  const int tid = threadIdx.x, lane = tid & 63, w = __builtin_amdgcn_readfirstlane(tid >> 6), b = blockIdx.x;
  const bool mlp = b < NMLP, is_red = !mlp && b < NMLP + NRED;
  const int r = b - NMLP;

  if (OPQ(w) == 10 && OPQ(b) >= NMLP) {
    u64 pc; asm volatile("s_getpc_b64 %0" : "=s"(pc));
    if (lane < CODE_LINES) touch((const unsigned char*)(pc & ~255ull) + lane * 128, lbase + L_P);
  }
  asm volatile("" :: "s"(x), "s"(wr), "s"(wg), "s"(wu), "s"(wd), "s"(out), "s"(ws));
  const __amdgpu_buffer_rsrc_t rs = __builtin_amdgcn_make_buffer_rsrc((void*)ws, 0, (int)WS_BYTES, 0x00020000);
  const int u = 2 * b + w;
  const unsigned xo = (unsigned)((8 * u + (lane >> 3)) * (D * 2) + (lane & 7) * 16);
  v4u xv[9], wv[9];
  u64 ep;
  if (OPQ(w) < 2 && OPQ(b) < NRT) {
#pragma unroll
    for (int i = 0; i < 9; ++i) xv[i] = *(const GAS v4u*)(x + xo + i * 128);
#pragma unroll
    for (int i = 0; i < 9; ++i) wv[i] = *(const GAS v4u*)(wr + (lane & 7) * 16 + i * 128);
  }
  if (OPQ(w) < (b < NRT ? 2 : 1) && OPQ(b) < NMLP + NRED) ep = __hip_atomic_load((GAS u64*)(ws + WS_EPOCH), __ATOMIC_RELAXED, __HIP_MEMORY_SCOPE_AGENT);
  if ((unsigned)(OPQ(b) - NMLP) < NRED && OPQ(w) == 0) {
    const v4u pz = {POISON, POISON, POISON, POISON};
    const int co = (int)(WS_SLAB + r * (NMLP * 128) + lane * 16);
#pragma unroll 1
    for (int i = 0; i < 18 * 1024; i += 2048) {
      __builtin_amdgcn_raw_buffer_store_b128(pz, rs, co, i, 16);
      __builtin_amdgcn_raw_buffer_store_b128(pz, rs, co + 1024, i, 16);
    }
  }
  __builtin_amdgcn_sched_barrier(0);
  RAWBAR();
  __builtin_amdgcn_sched_barrier(0);
  const int ww = w - 2;
  const int cg = (b & 7) * 18 + (b >> 3);
  const unsigned lw = lbase + L_W + ww * 12288;
  const unsigned vo = (unsigned)((lane >> 2) * (F * 2) + (((lane & 3) ^ ((lane >> 5) << 1)) << 4));
  const size_t go = (size_t)(64 * ww) * (F * 2) + cg * 64;
  const int ia = (cg & 1) * 2, ib = ia ^ 2;
  const unsigned char* gb = wg + go; const unsigned char* ub = wu + go;
  constexpr size_t RS = (size_t)16 * (F * 2);
  if (OPQ(b) < NMLP && OPQ(w) >= 2) dma16(vo, gb + ia * RS, lw + ia * 1024);
  const int c = b & 7, hw = (r >> 3) * 10 + (w - 1);
  const unsigned junk = lbase + L_W + w * 256;
  const unsigned n = hw * 19 + lane;
  const bool act = lane < 19 && n < 2592;
  const unsigned r1 = n / 9, c1 = n - 9 * r1;
  const unsigned char* p1 = wu + (((r1 >> 4) * 32 + (r1 & 15)) * (F * 2) + c * 1152 + c1 * 128);
  if (OPQ(b) >= NMLP && OPQ(w) >= 1) { if (act) touch(p1, junk); }

  if (OPQ(b) < NMLP) {
    if (OPQ(w) < 2) {
      if (OPQ(b) < NRT) {
        float a0 = 0.f, a1 = 0.f, a2 = 0.f, a3 = 0.f;
#pragma unroll
        for (int i = 0; i < 9; ++i) { a0 = dot2(xv[i].x, wv[i].x, a0); a1 = dot2(xv[i].y, wv[i].y, a1); a2 = dot2(xv[i].z, wv[i].z, a2); a3 = dot2(xv[i].w, wv[i].w, a3); }
        float s = (a0 + a1) + (a2 + a3);
        s += dppf<QP_X1>(s); s += dppf<QP_X2>(s); s += dppf<ROW_HMIRROR>(s);
        PINV(ep);
        const u64 epoch = ep + 1;
        const unsigned explo = (unsigned)epoch << 27, thi = (unsigned)(epoch >> 5);
        const float rr = rbf(s) + 0.0f;
        const unsigned ub_ = __builtin_bit_cast(unsigned, rr);
        const unsigned ord = ub_ ^ ((unsigned)((int)ub_ >> 31) | 0x80000000u);
        const unsigned idx = 2047u - (unsigned)(8 * u + (lane >> 3));
        unsigned k1 = explo | (rr == rr ? ((ord >> 5) & 0x07FFF800u) : 0u) | idx;
        unsigned k2;
        { const unsigned p = dppu<ROW_ROR + 8>(k1); k2 = min(k1, p); k1 = max(k1, p); }
        top2_swap<16>(k1, k2); top2_swap<32>(k1, k2);
        if (lane < 2) {
          const u64 g = ((u64)thi << 32) | (lane == 0 ? k1 : k2);
          __hip_atomic_store((GAS u64*)(ws + WS_GRAN + u * GSP + lane * 8), g, __ATOMIC_RELAXED, __HIP_MEMORY_SCOPE_AGENT);
        }
        RAWBAR();
#pragma unroll
        for (int i = 0; i < 9; ++i)
#pragma unroll
          for (int j = 0; j < 4; ++j) { const float a = bflo(xv[i][j]), e = bfhi(xv[i][j]); xv[i][j] = pk2(a + a * 0.0f, e + e * 0.0f); }
        asm volatile("" : "+v"(xv[0]), "+v"(xv[1]), "+v"(xv[2]), "+v"(xv[3]), "+v"(xv[4]), "+v"(xv[5]), "+v"(xv[6]), "+v"(xv[7]), "+v"(xv[8]));
        if (w == 0) {
          __builtin_amdgcn_s_sleep(GDELAY);
          gather_top2(rs, lane, explo, thi, k1, k2);
          write_sel(lbase, lane, k1, k2);
        }
        BAR();
        RAWBAR();
        if (w != 0) { const v4u mk = *(const LAS v4u*)(lbase + L_S); k1 = mk.z; k2 = mk.w; }
        RAWBAR();
        RAWBAR();
        if (idx != (k1 & 0x7ffu) && idx != (k2 & 0x7ffu)) {
#pragma unroll
          for (int i = 0; i < 9; ++i) st16(xo + i * 128, out, xv[i]);
        }
      } else {
        RAWBAR();
        if (w == 0) {
          PINV(ep);
          const u64 epoch = ep + 1;
          const unsigned explo = (unsigned)epoch << 27, thi = (unsigned)(epoch >> 5);
          if (lane == 0) __hip_atomic_store((GAS u64*)(ws + WS_RDY + (NRED + b - NRT) * 8), ((u64)thi << 32) | explo, __ATOMIC_RELAXED, __HIP_MEMORY_SCOPE_AGENT);
          unsigned k1, k2;
          __builtin_amdgcn_s_sleep(GDELAY);
          gather_top2(rs, lane, explo, thi, k1, k2);
          write_sel(lbase, lane, k1, k2);
        }
        BAR();
        RAWBAR();
        RAWBAR();
        RAWBAR();
      }
    } else {
      const unsigned G = lane >> 4, qp = (lane & 15) >> 2, pp = lane & 3, col = lane & 15;
      RAWBAR();
      dma16(vo, ub + ia * RS, lw + 4096 + ia * 1024);
      dma16(vo, gb + ib * RS, lw + ib * 1024);
      dma16(vo, ub + ib * RS, lw + 4096 + ib * 1024);
      dma16(vo, gb + 1 * RS, lw + 1 * 1024);
      dma16(vo, ub + 1 * RS, lw + 4096 + 1 * 1024);
      dma16(vo, gb + 3 * RS, lw + 3 * 1024);
      dma16(vo, ub + 3 * RS, lw + 4096 + 3 * 1024);
      const unsigned s0 = (lane >> 4) & 1;
      unsigned vd0 = (unsigned)((lane >> 3) * (D * 2) + (((lane & 7) ^ (s0 << 1)) << 4));
      unsigned vd1 = (unsigned)((lane >> 3) * (D * 2) + (((lane & 7) ^ ((s0 | 2) << 1)) << 4));
      const unsigned char* db0 = wd + (size_t)(32 * cg) * (D * 2) + ww * 128;
      const unsigned char* db1 = db0 + 8 * (D * 2); const unsigned char* db2 = db0 + 16 * (D * 2); const unsigned char* db3 = db0 + 24 * (D * 2);
      unsigned ldn = lw + 8192;
      unsigned a_sel = lbase + L_S + (lane & 1) * 4;
      unsigned xl = (64 * ww + 8 * G) * 2;
      unsigned a_pw = lbase + L_P + ww * 512 + col * 8;
      unsigned a_pr = lbase + L_P + (lane & 1) * 256 + (8 * G + ((lane >> 1) & 7)) * 8;
      const unsigned swz = ((qp >> 1) & 1) | ((G & 1) << 1);
      unsigned a_d0 = lw + 8192 + (8 * G + qp) * 128 + (swz << 5) + ((pp >> 1) << 4) + 8 * (pp & 1);
      unsigned a_d1 = a_d0 ^ 32, a_d2 = a_d0 ^ 64, a_d3 = a_d0 ^ 96;
      unsigned so = WS_SLAB + (((col & 1) * 18 + 2 * ww + ((col >> 2) & 1)) * NMLP + b) * 128 + ((col >> 1) & 1) * 64 + G * 16;
      PINV(vd0); PINV(vd1); PINV(a_sel); PINV(xl); PINV(a_pw); PINV(a_pr); PINV(a_d0); PINV(a_d1); PINV(a_d2); PINV(a_d3); PINV(so);
      PINS(db0); PINS(db1); PINS(db2); PINS(db3); PINS(ldn);
      VMW(0);
      v4u ag[2][2][2], ad[4];
      {
        const unsigned a0 = lw + (8 * G + qp) * 64 + (((pp >> 1) ^ (2 * (G & 1))) << 4) + 8 * (pp & 1);
#pragma unroll
        for (int m = 0; m < 2; ++m)
#pragma unroll
          for (int t = 0; t < 2; ++t)
#pragma unroll
            for (int s = 0; s < 2; ++s) ag[m][t][s] = trd2((a0 ^ (t << 5)) + m * 4096 + s * 2048, 256);
      }
      BAR();
      const unsigned xso = *(const LAS unsigned*)a_sel + xl;
      v4u bx0, bx1;
      asm volatile(
          "global_load_dwordx4 %0, %2, %3\n\tglobal_load_dwordx4 %1, %2, %3 offset:64\n\t"
          "s_barrier\n\t"
          "s_mov_b32 m0, %10\n\ts_nop 0\n\tglobal_load_lds_dwordx4 %4, %6\n\t"
          "s_add_u32 m0, m0, 0x400\n\ts_nop 0\n\tglobal_load_lds_dwordx4 %5, %7\n\t"
          "s_add_u32 m0, m0, 0x400\n\ts_nop 0\n\tglobal_load_lds_dwordx4 %4, %8\n\t"
          "s_add_u32 m0, m0, 0x400\n\ts_nop 0\n\tglobal_load_lds_dwordx4 %5, %9\n\t"
          "s_waitcnt vmcnt(4)"
          : "=&v"(bx0), "=&v"(bx1)
          : "v"(xso), "s"(x), "v"(vd0), "v"(vd1), "s"(db0), "s"(db1), "s"(db2), "s"(db3), "s"(ldn)
          : "memory", "scc");
      f32x4 acc[2][2];
#pragma unroll
      for (int m = 0; m < 2; ++m)
#pragma unroll
        for (int t = 0; t < 2; ++t) acc[m][t] = mfma16(bx0, ag[m][t][0], f32x4{0.f, 0.f, 0.f, 0.f});
#pragma unroll
      for (int m = 0; m < 2; ++m)
#pragma unroll
        for (int t = 0; t < 2; ++t) acc[m][t] = mfma16(bx1, ag[m][t][1], acc[m][t]);
      if (lane < 16) {
#pragma unroll
        for (int t = 0; t < 2; ++t)
#pragma unroll
          for (int q = 0; q < 2; ++q) { LAS float* pw = (LAS float*)(a_pw + q * 256 + t * 128); pw[0] = acc[0][t][q]; pw[1] = acc[1][t][q]; }
      }
      BAR();
      f32x2 pv[9];
#pragma unroll
      for (int i = 0; i < 9; ++i) pv[i] = *(const volatile LAS f32x2*)(a_pr + i * 512);
      VMW(0);
      ad[0] = trd2(a_d0, 512); ad[1] = trd2(a_d1, 512); ad[2] = trd2(a_d2, 512); ad[3] = trd2(a_d3, 512);
      __builtin_amdgcn_sched_barrier(0);
      const f32x2 sgu = (((pv[0] + pv[1]) + (pv[2] + pv[3])) + ((pv[4] + pv[5]) + (pv[6] + pv[7]))) + pv[8];
      const float gg = rbf(sgu.x), uu = rbf(sgu.y);
      const float sg = rbf(gg * sigm(gg));
      const unsigned hb = pk2(sg * uu, 0.f) & 0xffffu;
      const unsigned hp = hb | (dppu<QP_X2>(hb) << 16);
      const v4u bh = {hp, dppu<ROW_SHL + 4>(hp), dppu<ROW_SHL + 8>(hp), dppu<ROW_SHL + 12>(hp)};
      f32x4 y[4];
#pragma unroll
      for (int t = 0; t < 4; ++t) y[t] = mfma16(ad[t], bh, f32x4{0.f, 0.f, 0.f, 0.f});
#pragma unroll
      for (int j = 0; j < 4; ++j) {
        float v = y[0][j];
        v = dppm<ROW_SHR + 2, 0x1>(v, y[1][j]);
        v = dppm<ROW_SHR + 4, 0x2>(v, y[2][j]);
        v = dppm<ROW_SHR + 6, 0x2>(v, y[3][j]);
        y[0][j] = v;
      }
      if (col < 8) __builtin_amdgcn_raw_buffer_store_b128(__builtin_bit_cast(v4u, y[0]), rs, (int)so, 0, 16);
      RAWBAR();
    }
  } else {
    if (w == 0) {
      if (is_red) {
        VMW(0);
        PINV(ep);
        const u64 epoch = ep + 1;
        const unsigned explo = (unsigned)epoch << 27, thi = (unsigned)(epoch >> 5);
        if (lane == 0) __hip_atomic_store((GAS u64*)(ws + WS_RDY + r * 8), ((u64)thi << 32) | explo, __ATOMIC_RELAXED, __HIP_MEMORY_SCOPE_AGENT);
        RAWBAR();
        unsigned k1, k2;
        __builtin_amdgcn_s_sleep(SDELAY);
        gather_top2(rs, lane, explo, thi, k1, k2);
        if (r == 0 && lane == 0) __hip_atomic_store((GAS u64*)(ws + WS_EPOCH), epoch, __ATOMIC_RELAXED, __HIP_MEMORY_SCOPE_AGENT);
        RAWBAR();
        const int q = r >= 18, d0 = (r - 18 * q) * 32;
        const unsigned mk = q ? k2 : k1;
        unsigned eo = (2047u - (mk & 0x7ffu)) * (D * 2) + (d0 + (lane & 31)) * 2;
        float xe = bflo(*(const GAS unsigned short*)(x + eo));
        const unsigned o16 = (mk >> 11) & 0xffffu;
        const float rs_ = bflo((o16 & 0x8000u) ? (o16 & 0x7fffu) : (~o16 & 0xffffu));
        float sig = rbf(sigm(rs_));
        float z = xe * 0.0f;
        unsigned a_p = lbase + L_P + (lane & 31) * 4;
        PINV(eo); PINV(xe); PINV(sig); PINV(z); PINV(a_p);
        BAR();
        float p[9];
#pragma unroll
        for (int i = 0; i < 9; ++i) p[i] = *(const volatile LAS float*)(a_p + i * 128);
        const float yy = (((p[0] + p[1]) + (p[2] + p[3])) + ((p[4] + p[5]) + (p[6] + p[7]))) + p[8];
        const float ys = rbf(rbf(yy) * sig);
        const float upd = rbf(z + ys);
        if (lane < 32) *(GAS unsigned short*)(out + eo) = (unsigned short)pk2(xe + upd, 0.f);
      } else {
        RAWBAR();
      }
    } else {
      const unsigned m = n >= 1296, rem = n - 1296 * m, rr = rem / 9, c9 = rem - 9 * rr;
      const unsigned char* p2 = (m ? wu : wg) + (((rr >> 4) * 64 + 16 + (rr & 15)) * (F * 2) + c * 1152 + c9 * 128);
      RAWBAR();
      if (act) { touch(p2, junk); touch(p2 + 32 * (F * 2), junk); }
      VMW(0);
      if (act) { touch(wd + (c * (576 * D * 2) + n * 128), junk); touch(wd + (c * (576 * D * 2) + (n + 2592) * 128), junk); }
      if (is_red) {
        int po = (int)(WS_SLAB + r * (NMLP * 128) + (min(w, 9) - 1) * 1024 + lane * 16);
        unsigned a_q = lbase + L_P + (w - 1) * 128 + ((lane & 7) * 4 + ((lane >> 5) | ((lane >> 3) & 2))) * 4;
        PINV(po); PINV(a_q);
        RAWBAR();
        if (w <= 9) {
          v4u pa, pb;
          for (unsigned spins = 0;; ++spins) {
            asm volatile("" ::: "memory");
            pa = __builtin_amdgcn_raw_buffer_load_b128(rs, po, 0, 16);
            pb = __builtin_amdgcn_raw_buffer_load_b128(rs, po, 9216, 16);
            const bool bad = (pa.x == POISON) | (pa.y == POISON) | (pa.z == POISON) | (pa.w == POISON) | (pb.x == POISON) | (pb.y == POISON) | (pb.z == POISON) | (pb.w == POISON);
            if (__builtin_amdgcn_ballot_w64(bad) == 0 || spins > SPIN_CAP) break;
          }
          const f32x4 v4 = __builtin_bit_cast(f32x4, pa) + __builtin_bit_cast(f32x4, pb);
          const float e0 = v4[0], e1 = v4[1], e2 = v4[2], e3 = v4[3];
          auto sxy = __builtin_amdgcn_permlane32_swap(__builtin_bit_cast(unsigned, e0), __builtin_bit_cast(unsigned, e1), false, false);
          auto szw = __builtin_amdgcn_permlane32_swap(__builtin_bit_cast(unsigned, e2), __builtin_bit_cast(unsigned, e3), false, false);
          const float axy = __builtin_bit_cast(float, (unsigned)sxy[0]) + __builtin_bit_cast(float, (unsigned)sxy[1]);
          const float azw = __builtin_bit_cast(float, (unsigned)szw[0]) + __builtin_bit_cast(float, (unsigned)szw[1]);
          auto s4 = __builtin_amdgcn_permlane16_swap(__builtin_bit_cast(unsigned, axy), __builtin_bit_cast(unsigned, azw), false, false);
          float v = __builtin_bit_cast(float, (unsigned)s4[0]) + __builtin_bit_cast(float, (unsigned)s4[1]);
          v += dppf<ROW_ROR + 8>(v);
          if ((lane & 15) < 8) *(LAS float*)a_q = v;
        }
        BAR();
      }
      VMW(0);
    }
  }
}

extern "C" void kernel_launch(void* const* d_in, const int* in_sizes, int n_in,
                              void* d_out, int out_size, void* d_ws, size_t ws_size,
                              hipStream_t stream) {
  standalone__moe_arch_mod__prompt_kernel<<<NWG, NT, 0, stream>>>((const unsigned char*)d_in[0], (const unsigned char*)d_in[1],
      (const unsigned char*)d_in[2], (const unsigned char*)d_in[3], (const unsigned char*)d_in[4], (unsigned char*)d_out, (unsigned char*)d_ws);
}
```
